```python
import torch

def _softmax(x, axis=-1):
    return torch.softmax(x, dim=axis)
def _gelu(x):
    return torch.nn.functional.gelu(x, approximate="tanh")
def _silu(x):
    return torch.nn.functional.silu(x)
def _sigmoid(x):
    return torch.sigmoid(x)
def _layernorm(x, axis=-1, eps=1e-5):
    m = x.mean(dim=axis, keepdim=True)
    v = x.var(dim=axis, keepdim=True, unbiased=False)
    return (x - m) / torch.sqrt(v + eps)
def _scatter_add(tgt, idx, src, axis=0):
    return tgt.clone().index_add_(axis, idx, src)
def _segment_sum(src, seg, n):
    out = torch.zeros((n,) + src.shape[1:], dtype=src.dtype)
    return out.index_add_(0, seg, src)
def _pad(x, pw):
    flat = tuple(v for pair in reversed(pw) for v in pair)
    return torch.nn.functional.pad(x, flat)

kernel_name = 'recurrent_scan__spectral_fnet__dense_activation_squared_relu__prompt'

def setup_inputs():

    _w = (lambda a: (lambda t: t.to(torch.bfloat16) if t.is_floating_point() else t)(torch.as_tensor(a)))
    def _A_setup():
        B, S, D = 1, 2048, 512
        ns = torch.arange(S)[:, None] * torch.arange(S)[None, :]
        nd = torch.arange(D)[:, None] * torch.arange(D)[None, :]
        Ws_re = torch.cos(-2.0 * torch.pi * ns / S).to(float)
        Ws_im = torch.sin(-2.0 * torch.pi * ns / S).to(float)
        Wd_re = torch.cos(-2.0 * torch.pi * nd / D).to(float)
        Wd_im = torch.sin(-2.0 * torch.pi * nd / D).to(float)
        return {
            "x": _w((lambda *s: torch.randn(*s, dtype=torch.bfloat16) * 0.05)(B, S, D)),
            "dft_seq_re": _w(Ws_re),
            "dft_seq_im": _w(Ws_im),
            "dft_hid_re": _w(Wd_re),
            "dft_hid_im": _w(Wd_im),
        }
    _A_in = _A_setup()

    def _B_setup():
        B, S, D, F = 1, 2048, 512, 1792
        return {
            "x": _w((lambda *s: torch.randn(*s, dtype=torch.bfloat16) * 0.05)(B, S, D)),
            "w1": _w((lambda *s: torch.randn(*s, dtype=torch.bfloat16) * 0.05)(D, F)),
            "b1": _w((lambda *s: torch.randn(*s, dtype=torch.bfloat16) * 0.4)(F)),
            "w2": _w((lambda *s: torch.randn(*s, dtype=torch.bfloat16) * 0.05)(F, D)),
            "b2": _w((lambda *s: torch.randn(*s, dtype=torch.bfloat16) * 0.8)(D)),
        }
    _B_in = _B_setup()

    return {
        "A_x": _A_in["x"],
        "A_dft_seq_re": _A_in["dft_seq_re"],
        "A_dft_seq_im": _A_in["dft_seq_im"],
        "A_dft_hid_re": _A_in["dft_hid_re"],
        "A_dft_hid_im": _A_in["dft_hid_im"],
        "B_x": _B_in["x"],
        "B_w1": _B_in["w1"],
        "B_b1": _B_in["b1"],
        "B_w2": _B_in["w2"],
        "B_b2": _B_in["b2"],
    }

def reference(A_x, A_dft_seq_re, A_dft_seq_im, A_dft_hid_re, A_dft_hid_im, B_x, B_w1, B_b1, B_w2, B_b2):
    def _A_ref(x, dft_seq_re, dft_seq_im, dft_hid_re, dft_hid_im):
        a = torch.einsum("bsd,ed->bse", x, dft_hid_re)
        b = torch.einsum("bsd,ed->bse", x, dft_hid_im)
        y = torch.einsum("ts,bse->bte", dft_seq_re, a) - torch.einsum("ts,bse->bte", dft_seq_im, b)
        return y

    def _B_ref(x, w1, b1, w2, b2):
        h = torch.einsum("bsd,df->bsf", x, w1) + b1
        zero = torch.zeros_like(h)
        r = torch.where(h > zero, h, zero)
        h = r * r
        out = torch.einsum("bsf,fd->bsd", h, w2) + b2
        return out

    x = A_x
    xn = _layernorm(x, axis=-1)
    s = _A_ref(xn, A_dft_seq_re, A_dft_seq_im, A_dft_hid_re, A_dft_hid_im)
    g = _silu(_B_ref(xn, B_w1, B_b1, B_w2, B_b2))
    return x + s * g
```

```cpp
#include <hip/hip_runtime.h>
#include <hip/hip_bf16.h>
#include <stdint.h>

typedef unsigned short u16;
__device__ __forceinline__ float bf2f(u16 v) { return __uint_as_float(((unsigned)v) << 16); }
__device__ __forceinline__ u16 f2bf(float f) { __hip_bfloat16 b = __float2bfloat16(f); return *reinterpret_cast<u16*>(&b); }
__device__ __forceinline__ float rbf(float f) { return bf2f(f2bf(f)); }

constexpr int S = 2048, D = 512, F = 1792;

__global__ void n_ln(const u16* x, u16* xn) {
  int row = blockIdx.x; int lane = threadIdx.x;
  float v[8]; float s = 0;
  for (int i = 0; i < 8; ++i) { v[i] = bf2f(x[row * D + lane * 8 + i]); s += v[i]; }
  for (int o = 32; o; o >>= 1) s += __shfl_xor(s, o);
  float m = s / D; float q = 0;
  for (int i = 0; i < 8; ++i) { float d = v[i] - m; q += d * d; }
  for (int o = 32; o; o >>= 1) q += __shfl_xor(q, o);
  float var = rbf(q / D); float mb = rbf(m);
  float den = rbf(sqrtf(rbf(var + 1e-5f)));
  for (int i = 0; i < 8; ++i) xn[row * D + lane * 8 + i] = f2bf(rbf(v[i] - mb) / den);
}
__global__ void n_gemm(const u16* A, const u16* Bp, float* C, int M, int N, int K, int sk, int sn) {
  int n = blockIdx.x * 64 + (threadIdx.x & 63); int m = blockIdx.y * 4 + (threadIdx.x >> 6);
  float acc = 0;
  for (int k = 0; k < K; ++k) acc += bf2f(A[(size_t)m * K + k]) * bf2f(Bp[(size_t)k * sk + (size_t)n * sn]);
  C[(size_t)m * N + n] = acc;
}
__global__ void n_tobf(const float* C, u16* o, int n) { int i = blockIdx.x * 256 + threadIdx.x; if (i < n) o[i] = f2bf(C[i]); }
__global__ void n_h(const float* C, const u16* b1, u16* o) {
  int i = blockIdx.x * 256 + threadIdx.x; int f = i % F;
  float h = rbf(rbf(C[i]) + bf2f(b1[f])); float r = h > 0.f ? h : 0.f; o[i] = f2bf(r * r);
}
__global__ void n_final(const float* y1, const float* y2, const float* o1, const u16* b2, const u16* x, u16* out) {
  int i = blockIdx.x * 256 + threadIdx.x; int d = i % D;
  float y = rbf(rbf(y1[i]) - rbf(y2[i]));
  float o = rbf(rbf(o1[i]) + bf2f(b2[d]));
  float g = rbf(o / (1.f + __expf(-o)));
  out[i] = f2bf(bf2f(x[i]) + rbf(y * g));
}

extern "C" void kernel_launch(void* const* d_in, const int* in_sizes, int n_in,
                              void* d_out, int out_size, void* d_ws, size_t ws_size,
                              hipStream_t stream) {
  const u16* x = (const u16*)d_in[0];
  const u16* cr = (const u16*)d_in[1];
  const u16* ci = (const u16*)d_in[2];
  const u16* dr = (const u16*)d_in[3];
  const u16* di = (const u16*)d_in[4];
  const u16* w1 = (const u16*)d_in[6];
  const u16* b1 = (const u16*)d_in[7];
  const u16* w2 = (const u16*)d_in[8];
  const u16* b2 = (const u16*)d_in[9];
  char* ws = (char*)d_ws;
  u16* xn = (u16*)ws;
  u16* a = (u16*)(ws + (2 << 20));
  u16* b = (u16*)(ws + (4 << 20));
  u16* h = (u16*)(ws + (6 << 20));
  float* t0 = (float*)(ws + (16 << 20));
  float* y1 = (float*)(ws + (32 << 20));
  float* y2 = (float*)(ws + (40 << 20));
  float* o1 = (float*)(ws + (48 << 20));
  n_ln<<<S, 64, 0, stream>>>(x, xn);
  n_gemm<<<dim3(D / 64, S / 4), 256, 0, stream>>>(xn, dr, t0, S, D, D, 1, D);
  n_tobf<<<S * D / 256, 256, 0, stream>>>(t0, a, S * D);
  n_gemm<<<dim3(D / 64, S / 4), 256, 0, stream>>>(xn, di, t0, S, D, D, 1, D);
  n_tobf<<<S * D / 256, 256, 0, stream>>>(t0, b, S * D);
  n_gemm<<<dim3(F / 64, S / 4), 256, 0, stream>>>(xn, w1, t0, S, F, D, F, 1);
  n_h<<<S * F / 256, 256, 0, stream>>>(t0, b1, h);
  n_gemm<<<dim3(D / 64, S / 4), 256, 0, stream>>>(cr, a, y1, S, D, S, D, 1);
  n_gemm<<<dim3(D / 64, S / 4), 256, 0, stream>>>(ci, b, y2, S, D, S, D, 1);
  n_gemm<<<dim3(D / 64, S / 4), 256, 0, stream>>>(h, w2, o1, S, D, F, D, 1);
  n_final<<<S * D / 256, 256, 0, stream>>>(y1, y2, o1, b2, x, (u16*)d_out);
}
```

```cpp
#include <hip/hip_runtime.h>
#include <hip/hip_bf16.h>
#include <stdint.h>

typedef unsigned short u16;
typedef __attribute__((ext_vector_type(8))) __bf16 bf16x8;
typedef __attribute__((ext_vector_type(2))) __bf16 bf16x2;
typedef __attribute__((ext_vector_type(4))) short s16x4;
typedef __attribute__((ext_vector_type(4))) float f32x4;
typedef __attribute__((ext_vector_type(2))) float f32x2;
typedef __attribute__((ext_vector_type(4))) unsigned u32x4;
typedef __attribute__((ext_vector_type(2))) unsigned u32x2;
#define LAS __attribute__((address_space(3)))
#define GAS __attribute__((address_space(1)))
#define DEVI __device__ __forceinline__

constexpr int S = 2048, D = 512, F = 1792;
constexpr int NFB = F / 32;
constexpr int NSB = S / 32;

constexpr size_t WS_ABF = 0;
constexpr size_t WS_HF = 4u << 20;
constexpr size_t WS_W2F = 12u << 20;
constexpr size_t WS_PART = 16u << 20;
constexpr size_t WS_CNT = 32u << 20;

DEVI float bf2f(u16 v) { return __uint_as_float(((unsigned)v) << 16); }
DEVI float bflo(unsigned v) { return __uint_as_float(v << 16); }
DEVI float bfhi(unsigned v) { return __uint_as_float(v & 0xffff0000u); }
DEVI unsigned pack2(float a, float b) {
  bf16x2 r; r[0] = (__bf16)a; r[1] = (__bf16)b; return __builtin_bit_cast(unsigned, r);
}
DEVI float rbf(float f) { return (float)(__bf16)f; }
DEVI float dot2(unsigned a, unsigned b, float c) {
  return __builtin_amdgcn_fdot2_f32_bf16(__builtin_bit_cast(bf16x2, a), __builtin_bit_cast(bf16x2, b), c, false);
}
DEVI f32x4 mfma16(u32x4 a, u32x4 b, f32x4 c) {
  return __builtin_amdgcn_mfma_f32_16x16x32_bf16(__builtin_bit_cast(bf16x8, a), __builtin_bit_cast(bf16x8, b), c, 0, 0, 0);
}
DEVI void glds16(unsigned voff, const void* sbase, unsigned lds) {
  unsigned keep;
  asm volatile("s_mov_b32 %0, m0\n\ts_mov_b32 m0, %3\n\ts_nop 0\n\tglobal_load_lds_dwordx4 %1, %2\n\ts_mov_b32 m0, %0"
               : "=&s"(keep) : "v"(voff), "s"(sbase), "s"(lds) : "memory");
}
#define WAITV(n) asm volatile("s_waitcnt vmcnt(" #n ")" ::: "memory")
#define BAR_LGKM() asm volatile("s_waitcnt lgkmcnt(0)\n\ts_barrier" ::: "memory")
#define SCHED0() __builtin_amdgcn_sched_barrier(0)
DEVI u32x4 ldsr128(unsigned a) { return *(const LAS u32x4*)(uintptr_t)a; }
DEVI u32x2 ldstr(unsigned a) {
  return __builtin_bit_cast(u32x2, __builtin_amdgcn_ds_read_tr16_b64_v4i16((LAS s16x4*)(uintptr_t)a));
}
DEVI int perm32(int r) { return (r & ~31) | (((r >> 2) & 3) << 3) | (((r >> 4) & 1) << 2) | (r & 3); }

constexpr int K1_XB = 16384, K1_DB = 8192, K1_WB = 14336, K1_ST = K1_XB + K1_DB + K1_WB;
constexpr int K1_NS = 4;
constexpr int K1_TAB = K1_NS * K1_ST;
constexpr int K1_T_ROW = K1_TAB;
constexpr int K1_T_CD = K1_TAB + 1024;
constexpr int K1_T_CW = K1_TAB + 1024 + 256;
constexpr int K1_LDS = K1_T_CW + 4 * 112 * 4;

__global__ __launch_bounds__(512, 2) void k1_kernel(const u16* __restrict__ x, const u16* __restrict__ dr,
                                                    const u16* __restrict__ di, const u16* __restrict__ w1,
                                                    const u16* __restrict__ b1, const u16* __restrict__ w2,
                                                    char* __restrict__ ws) {
  __shared__ __attribute__((aligned(16))) char smem[K1_LDS];
  const unsigned lbase = (unsigned)(uintptr_t)(LAS char*)smem;
  const int bid = blockIdx.x;
  const int xcd = bid & 7, li = bid >> 3;
  const int r = (xcd & 1) * 8 + (li & 7);
  const int c = (xcd >> 1) * 4 + (li >> 3);
  const int wave = __builtin_amdgcn_readfirstlane(threadIdx.x >> 6);
  const int lane = threadIdx.x & 63;
  const int codd = c & 1;

  if (wave >= 4) {
    const int lw = wave - 4;
    unsigned vx[4], vd[2], vw[4];
#pragma unroll
    for (int i = 0; i < 4; ++i) {
      int pc = lw * 4 + i; int rho = pc * 8 + (lane >> 3); int ch = (lane & 7) ^ ((rho >> 1) & 7);
      vx[i] = (unsigned)((r * 128 + perm32(rho)) * D + ch * 8) * 2u;
    }
#pragma unroll
    for (int i = 0; i < 2; ++i) {
      int pc = lw * 2 + i; int rho = pc * 8 + (lane >> 3); int ch = (lane & 7) ^ ((rho >> 1) & 7);
      vd[i] = (unsigned)(((c & 7) * 64 + perm32(rho)) * D + ch * 8) * 2u;
    }
    const int wp0 = lw < 2 ? lw * 4 : 8 + (lw - 2) * 3;
    const int wn = lw < 2 ? 4 : 3;
    int wpc[4];
#pragma unroll
    for (int i = 0; i < 4; ++i) {
      int pc = wp0 + (i < wn ? i : 0); wpc[i] = pc;
      int o = pc * 1024 + lane * 16; int rho = o / 224; int cp = (o - rho * 224) >> 4;
      int k = (rho & ~12) | ((rho & 4) << 1) | ((rho & 8) >> 1);
      int tl = (cp >> 1) + codd; if (tl == 7) tl = 0;
      vw[i] = (unsigned)(k * F + c * 112 + tl * 16 + (cp & 1) * 8) * 2u;
    }
    const u16* dm = c < 8 ? dr : di;
    auto issue = [&](int j) {
      const unsigned sl = lbase + (unsigned)(j % K1_NS) * K1_ST;
      const char* bx = (const char*)x + j * 128;
      const char* bd = (const char*)dm + j * 128;
      const char* bw = (const char*)w1 + (size_t)j * 64 * F * 2;
#pragma unroll
      for (int i = 0; i < 4; ++i) glds16(vx[i], bx, sl + (lw * 4 + i) * 1024);
#pragma unroll
      for (int i = 0; i < 2; ++i) glds16(vd[i], bd, sl + K1_XB + (lw * 2 + i) * 1024);
#pragma unroll
      for (int i = 0; i < 4; ++i) glds16(vw[i], bw, sl + K1_XB + K1_DB + wpc[i] * 1024);
    };
    issue(0); issue(1); issue(2);
    float rs = 0.f, rq = 0.f, cd = 0.f, cw[7];
#pragma unroll
    for (int i = 0; i < 7; ++i) cw[i] = 0.f;
    const unsigned ONES = 0x3f803f80u;
    const unsigned ax = (unsigned)((lw * 32 + (lane >> 1)) * 128 + (lane & 1) * 64);
    const unsigned ad = (unsigned)(K1_XB + (lw * 16 + (lane >> 2)) * 128 + (lane & 3) * 32);
    const unsigned aw = (unsigned)(K1_XB + K1_DB + (lw * 16 + (lane >> 2)) * 224 + (lane & 3) * 8);
#pragma unroll
    for (int j = 0; j < 8; ++j) {
      if (j <= 5) WAITV(20); else if (j == 6) WAITV(10); else WAITV(0);
      BAR_LGKM();
      if (j + 3 < 8) issue(j + 3);
      const unsigned sl = lbase + (unsigned)(j % K1_NS) * K1_ST;
#pragma unroll
      for (int i = 0; i < 4; ++i) {
        u32x4 v = ldsr128(sl + ax + (unsigned)(((i + (lane >> 2)) & 3) * 16));
#pragma unroll
        for (int e = 0; e < 4; ++e) { rs = dot2(v[e], ONES, rs); rq = dot2(v[e], v[e], rq); }
      }
#pragma unroll
      for (int i = 0; i < 2; ++i) {
        u32x4 v = ldsr128(sl + ad + (unsigned)((i ^ ((lane >> 3) & 1)) * 16));
#pragma unroll
        for (int e = 0; e < 4; ++e) cd = dot2(v[e], ONES, cd);
      }
#pragma unroll
      for (int t = 0; t < 7; ++t) {
        u32x2 v = ldstr(sl + aw + t * 32);
        cw[t] = dot2(v[0], ONES, cw[t]); cw[t] = dot2(v[1], ONES, cw[t]);
      }
    }
    rs += __shfl_xor(rs, 1); rq += __shfl_xor(rq, 1);
    float mean = rs * (1.f / D);
    float var = fmaxf(rq * (1.f / D) - mean * mean, 0.f);
    float den = rbf(sqrtf(rbf(rbf(var) + 1e-5f)));
    if ((lane & 1) == 0) *(LAS f32x2*)(uintptr_t)(lbase + K1_T_ROW + (lw * 32 + (lane >> 1)) * 8) = (f32x2){rbf(mean), 1.f / den};
    cd += __shfl_xor(cd, 1); cd += __shfl_xor(cd, 2);
    if ((lane & 3) == 0) *(LAS float*)(uintptr_t)(lbase + K1_T_CD + (lw * 16 + (lane >> 2)) * 4) = cd;
#pragma unroll
    for (int t = 0; t < 7; ++t) {
      float v = cw[t]; v += __shfl_xor(v, 16); v += __shfl_xor(v, 32);
      if (lane < 16) *(LAS float*)(uintptr_t)(lbase + K1_T_CW + (lw * 112 + t * 16 + lane) * 4) = v;
    }
    BAR_LGKM();
    return;
  }

  const int w = wave;
  const int fi = lane & 15, g = lane >> 4;
  if (threadIdx.x == 0) *(unsigned*)(ws + WS_CNT + bid * 128) = 0u;
  {
#pragma unroll
    for (int n = 0; n < 2; ++n) {
      int nn = w * 2 + n;
      if (nn < 7) {
        int phi = bid * 7 + nn; int dt = phi / NFB, fb = phi - dt * NFB;
        int d = (dt >> 1) * 32 + (fi >> 2) * 8 + (dt & 1) * 4 + (fi & 3);
        const u16* p = w2 + (size_t)(fb * 32 + g * 4) * D + d;
        u32x4 o;
        o[0] = p[0] | ((unsigned)p[D] << 16);
        o[1] = p[2 * D] | ((unsigned)p[3 * D] << 16);
        o[2] = p[16 * D] | ((unsigned)p[17 * D] << 16);
        o[3] = p[18 * D] | ((unsigned)p[19 * D] << 16);
        *(u32x4*)(ws + WS_W2F + (size_t)phi * 1024 + lane * 16) = o;
      }
    }
  }
  u32x2 b1v[7];
#pragma unroll
  for (int t = 0; t < 7; ++t) {
    int tl = t + codd; if (tl == 7) tl = 0;
    b1v[t] = *(const u32x2*)(b1 + c * 112 + tl * 16 + g * 4);
  }
  f32x4 aab[4][2], ah[7][2];
#pragma unroll
  for (int n = 0; n < 4; ++n) { aab[n][0] = (f32x4){0, 0, 0, 0}; aab[n][1] = (f32x4){0, 0, 0, 0}; }
#pragma unroll
  for (int n = 0; n < 7; ++n) { ah[n][0] = (f32x4){0, 0, 0, 0}; ah[n][1] = (f32x4){0, 0, 0, 0}; }
  const unsigned sw = (unsigned)(fi >> 1);
  const unsigned ar0 = lbase + (unsigned)(fi * 128) + ((g ^ sw) * 16);
  const unsigned ar1 = lbase + (unsigned)(fi * 128) + (((g ^ sw) ^ 4) * 16);
  const unsigned axw = (unsigned)(w * 4096);
  const unsigned atr = lbase + K1_XB + K1_DB + (unsigned)(((g >> 1) * 16 + (g & 1) * 4 + (fi >> 2)) * 224 + (fi & 3) * 8);

  u32x4 xf[2][2], df[2][4], wf[2][7];
  auto loadf = [&](int buf, unsigned so, int kk) {
    unsigned ar = (kk ? ar1 : ar0) + so, at = atr + so;
    asm volatile("" : "+v"(ar), "+v"(at));
    unsigned arx = ar + axw;
#pragma unroll
    for (int u = 0; u < 2; ++u) xf[buf][u] = ldsr128(arx + u * 2048);
#pragma unroll
    for (int n = 0; n < 4; ++n) df[buf][n] = ldsr128(ar + K1_XB + n * 2048);
#pragma unroll
    for (int t = 0; t < 7; ++t) {
      u32x2 lo = ldstr(at + (kk * 32) * 224 + t * 32);
      u32x2 hi = ldstr(at + (kk * 32 + 8) * 224 + t * 32);
      wf[buf][t] = (u32x4){lo[0], lo[1], hi[0], hi[1]};
    }
  };
  auto domma = [&](int buf) {
#pragma unroll
    for (int n = 0; n < 4; ++n)
#pragma unroll
      for (int u = 0; u < 2; ++u) aab[n][u] = mfma16(xf[buf][u], df[buf][n], aab[n][u]);
#pragma unroll
    for (int t = 0; t < 7; ++t)
#pragma unroll
      for (int u = 0; u < 2; ++u) ah[t][u] = mfma16(wf[buf][t], xf[buf][u], ah[t][u]);
  };
  auto interleave = [&]() {
#pragma unroll
    for (int i = 0; i < 20; ++i) {
      __builtin_amdgcn_sched_group_barrier(0x100, 1, 0);
      __builtin_amdgcn_sched_group_barrier(0x8, 1, 0);
    }
    __builtin_amdgcn_sched_group_barrier(0x8, 2, 0);
  };
  BAR_LGKM();
  loadf(0, 0, 0);
  SCHED0();
  {
    unsigned so = 0;
#pragma nounroll
    for (int j = 0; j < 8; ++j) {
      loadf(1, so, 1);
      domma(0);
      interleave();
      SCHED0();
      BAR_LGKM();
      so += K1_ST; if (so == K1_NS * K1_ST) so = 0;
      loadf(0, so, 0);
      domma(1);
      interleave();
      SCHED0();
    }
  }
  {
    f32x2 st[2][4];
#pragma unroll
    for (int u = 0; u < 2; ++u)
#pragma unroll
      for (int j = 0; j < 4; ++j) st[u][j] = *(const LAS f32x2*)(uintptr_t)(lbase + K1_T_ROW + (w * 32 + u * 16 + g * 4 + j) * 8);
    char* ob = ws + WS_ABF + ((size_t)(c * 4) * NSB + (r * 4 + w)) * 1024 + lane * 16;
#pragma unroll
    for (int n = 0; n < 4; ++n) {
      float cs = *(const LAS float*)(uintptr_t)(lbase + K1_T_CD + (n * 16 + fi) * 4);
      float v[8];
#pragma unroll
      for (int u = 0; u < 2; ++u)
#pragma unroll
        for (int j = 0; j < 4; ++j) v[u * 4 + j] = (aab[n][u][j] - st[u][j][0] * cs) * st[u][j][1];
      u32x4 o = {pack2(v[0], v[1]), pack2(v[2], v[3]), pack2(v[4], v[5]), pack2(v[6], v[7])};
      *(u32x4*)(ob + (size_t)n * NSB * 1024) = o;
    }
  }
  {
    f32x2 st[2];
#pragma unroll
    for (int u = 0; u < 2; ++u) st[u] = *(const LAS f32x2*)(uintptr_t)(lbase + K1_T_ROW + (w * 32 + u * 16 + fi) * 8);
    u32x2 hv[7][2];
#pragma unroll
    for (int t = 0; t < 7; ++t) {
      f32x4 cs = *(const LAS f32x4*)(uintptr_t)(lbase + K1_T_CW + (t * 16 + g * 4) * 4);
#pragma unroll
      for (int q = 1; q < 4; ++q) cs += *(const LAS f32x4*)(uintptr_t)(lbase + K1_T_CW + (q * 112 + t * 16 + g * 4) * 4);
      float bb[4] = {bflo(b1v[t][0]), bfhi(b1v[t][0]), bflo(b1v[t][1]), bfhi(b1v[t][1])};
#pragma unroll
      for (int u = 0; u < 2; ++u) {
        float v[4];
#pragma unroll
        for (int j = 0; j < 4; ++j) {
          float hh = rbf(rbf((ah[t][u][j] - st[u][0] * cs[j]) * st[u][1]) + bb[j]);
          hh = hh > 0.f ? hh : 0.f;
          v[j] = hh * hh;
        }
        hv[t][u] = (u32x2){pack2(v[0], v[1]), pack2(v[2], v[3])};
      }
    }
    const int fb0 = (c * 7 + codd) >> 1;
    const int fbs = codd ? (c * 7) >> 1 : (c * 7 + 6) >> 1;
#pragma unroll
    for (int u = 0; u < 2; ++u) {
      int t = r * 128 + w * 32 + (fi >> 2) * 8 + u * 4 + (fi & 3);
      char* hb = ws + WS_HF + (size_t)(t >> 4) * NFB * 1024 + (g * 16 + (t & 15)) * 16;
#pragma unroll
      for (int pr = 0; pr < 3; ++pr) {
        u32x4 o = {hv[2 * pr][u][0], hv[2 * pr][u][1], hv[2 * pr + 1][u][0], hv[2 * pr + 1][u][1]};
        *(u32x4*)(hb + (size_t)(fb0 + pr) * 1024) = o;
      }
      *(u32x2*)(hb + (size_t)fbs * 1024 + codd * 8) = hv[6][u];
    }
  }
}

constexpr int K2_ST = 32768, K2_NS = 4;
constexpr int K2_OWN = K2_NS * K2_ST;
constexpr int K2_LDS = K2_OWN + 16384;
constexpr int K2_NA = 16, K2_NB = 14, K2_N = K2_NA + K2_NB;

__global__ __launch_bounds__(512, 2) void k2_kernel(const u16* __restrict__ x, const u16* __restrict__ cr,
                                                    const u16* __restrict__ ci, const u16* __restrict__ b2,
                                                    char* __restrict__ ws, u16* __restrict__ out) {
  __shared__ __attribute__((aligned(16))) char smem[K2_LDS];
  const unsigned lbase = (unsigned)(uintptr_t)(LAS char*)smem;
  const int bid = blockIdx.x;
  const int xcd = bid & 7, li = bid >> 3;
  const int p = xcd >> 1;
  const int tt = (xcd & 1) * 8 + (li & 7);
  const int te = li >> 3;
  const int T = tt * 4 + te;
  const int wave = __builtin_amdgcn_readfirstlane(threadIdx.x >> 6);
  const int lane = threadIdx.x & 63;
  const int t0 = tt * 128 + (p >> 1) * 64, e0 = te * 128 + (p & 1) * 64;

  if (wave >= 4) {
    const int lw = wave - 4;
    unsigned vc[4], va[4], vh[4];
#pragma unroll
    for (int i = 0; i < 4; ++i) {
      int pc = lw * 4 + i; int rho = pc * 8 + (lane >> 3); int ch = (lane & 7) ^ ((rho >> 1) & 7);
      vc[i] = (unsigned)((tt * 128 + rho) * S + (p & 1) * 1024 + ch * 8) * 2u;
      va[i] = (unsigned)((((p >> 1) * 32 + te * 8 + (pc >> 1)) * NSB + (p & 1) * 32 + (pc & 1)) * 1024 + lane * 16);
      vh[i] = (unsigned)((lw * NFB + i) * 1024 + lane * 16);
    }
    const char* cm = (const char*)(p < 2 ? cr : ci);
    const char* abf = ws + WS_ABF;
    const char* hf = ws + WS_HF + (size_t)(t0 >> 4) * NFB * 1024;
    const char* w2f = ws + WS_W2F + (size_t)(e0 >> 4) * NFB * 1024;
    auto issue = [&](int j) {
      const unsigned sl = lbase + (unsigned)(j % K2_NS) * K2_ST + lw * 4096;
      if (j < K2_NA) {
        const char* b0 = cm + j * 128;
        const char* b1 = abf + j * 2048;
#pragma unroll
        for (int i = 0; i < 4; ++i) glds16(vc[i], b0, sl + i * 1024);
#pragma unroll
        for (int i = 0; i < 4; ++i) glds16(va[i], b1, sl + 16384 + i * 1024);
      } else {
        const char* b0 = hf + (j - K2_NA) * 4096;
        const char* b1 = w2f + (j - K2_NA) * 4096;
#pragma unroll
        for (int i = 0; i < 4; ++i) glds16(vh[i], b0, sl + i * 1024);
#pragma unroll
        for (int i = 0; i < 4; ++i) glds16(vh[i], b1, sl + 16384 + i * 1024);
      }
    };
    issue(0); issue(1); issue(2);
    for (int j = 0; j < K2_N - 3; ++j) {
      WAITV(16);
      BAR_LGKM();
      issue(j + 3);
    }
    WAITV(16); BAR_LGKM();
    WAITV(8); BAR_LGKM();
    WAITV(0); BAR_LGKM();
    return;
  }

  const int w = wave;
  const int fi = lane & 15, g = lane >> 4;
  const unsigned sw = (unsigned)(fi >> 1);
  const __amdgpu_buffer_rsrc_t prs = __builtin_amdgcn_make_buffer_rsrc(ws + WS_PART, 0, 16 << 20, 0x00020000);
  GAS unsigned* cnt = (GAS unsigned*)(ws + WS_CNT);
  {
    f32x4 acc[4][4];
#pragma unroll
    for (int a = 0; a < 4; ++a)
#pragma unroll
      for (int b = 0; b < 4; ++b) acc[a][b] = (f32x4){0, 0, 0, 0};
    const unsigned ac0 = lbase + (unsigned)(((w >> 1) * 64 + fi) * 128) + ((g ^ sw) * 16);
    const unsigned ac1 = lbase + (unsigned)(((w >> 1) * 64 + fi) * 128) + (((g ^ sw) ^ 4) * 16);
    const unsigned aa = lbase + 16384 + (unsigned)((w & 1) * 8192 + lane * 16);
    u32x4 cf[2][4], af[2][4];
    auto loadf = [&](int buf, unsigned so, int kk) {
      unsigned a0 = aa + so, a1 = (kk ? ac1 : ac0) + so;
      asm volatile("" : "+v"(a0), "+v"(a1));
#pragma unroll
      for (int n = 0; n < 4; ++n) { af[buf][n] = ldsr128(a0 + n * 2048 + kk * 1024); cf[buf][n] = ldsr128(a1 + n * 2048); }
    };
    auto domma = [&](int buf) {
#pragma unroll
      for (int a = 0; a < 4; ++a)
#pragma unroll
        for (int b = 0; b < 4; ++b) acc[a][b] = mfma16(af[buf][a], cf[buf][b], acc[a][b]);
    };
    auto interleave = [&]() {
#pragma unroll
      for (int i = 0; i < 8; ++i) {
        __builtin_amdgcn_sched_group_barrier(0x100, 1, 0);
        __builtin_amdgcn_sched_group_barrier(0x8, 1, 0);
      }
      __builtin_amdgcn_sched_group_barrier(0x8, 8, 0);
    };
    BAR_LGKM();
    loadf(0, 0, 0);
    SCHED0();
    unsigned so = 0;
#pragma nounroll
    for (int j = 0; j < K2_NA; ++j) {
      loadf(1, so, 1);
      domma(0);
      interleave();
      SCHED0();
      BAR_LGKM();
      so = (so + K2_ST) & (K2_NS * K2_ST - 1);
      loadf(0, so, 0);
      domma(1);
      interleave();
      SCHED0();
    }
    if (w == p) {
#pragma unroll
      for (int a = 0; a < 4; ++a)
#pragma unroll
        for (int b = 0; b < 4; ++b) *(LAS f32x4*)(uintptr_t)(lbase + K2_OWN + (a * 4 + b) * 1024 + lane * 16) = acc[a][b];
    } else {
      const unsigned po = (unsigned)(((T * 4 + w) * 4 + p) * 16384 + lane * 16);
#pragma unroll
      for (int a = 0; a < 4; ++a)
#pragma unroll
        for (int b = 0; b < 4; ++b)
          __builtin_amdgcn_raw_buffer_store_b128(__builtin_bit_cast(u32x4, acc[a][b]), prs, po + (a * 4 + b) * 1024, 0, 16);
    }
  }
  f32x4 acc[2][2];
#pragma unroll
  for (int a = 0; a < 2; ++a)
#pragma unroll
    for (int b = 0; b < 2; ++b) acc[a][b] = (f32x4){0, 0, 0, 0};
  const unsigned ah = lbase + (unsigned)((w >> 1) * 8192 + lane * 16);
  const unsigned aw = lbase + 16384 + (unsigned)((w & 1) * 8192 + lane * 16);
  u32x4 hf[2][2][2], wf[2][2][2];
  auto loadf = [&](int buf, int sl, int half) {
    unsigned a0 = aw + (unsigned)sl * K2_ST, a1 = ah + (unsigned)sl * K2_ST;
    asm volatile("" : "+v"(a0), "+v"(a1));
#pragma unroll
    for (int k = 0; k < 2; ++k)
#pragma unroll
      for (int n = 0; n < 2; ++n) {
        wf[buf][k][n] = ldsr128(a0 + n * 4096 + (half * 2 + k) * 1024);
        hf[buf][k][n] = ldsr128(a1 + n * 4096 + (half * 2 + k) * 1024);
      }
  };
  auto domma = [&](int buf) {
#pragma unroll
    for (int k = 0; k < 2; ++k)
#pragma unroll
      for (int a = 0; a < 2; ++a)
#pragma unroll
        for (int b = 0; b < 2; ++b) acc[a][b] = mfma16(wf[buf][k][a], hf[buf][k][b], acc[a][b]);
  };
  u32x4 pp[3][2][2], xv[2], b2v;
  unsigned seen = 0;
  loadf(0, 0, 0);
#pragma unroll
  for (int j = 0; j < K2_NB; ++j) {
    loadf(1, j & 3, 1);
    domma(0);
    SCHED0();
    if (j == 3) WAITV(0);
    if (j < K2_NB - 1) BAR_LGKM();
    if (j == 3 && threadIdx.x < 4 && (int)threadIdx.x != p)
      __hip_atomic_fetch_add(cnt + (T * 4 + threadIdx.x) * 32, 1u, __ATOMIC_RELAXED, __HIP_MEMORY_SCOPE_AGENT);
    if (j == 8) seen = __hip_atomic_load(cnt + (T * 4 + p) * 32, __ATOMIC_RELAXED, __HIP_MEMORY_SCOPE_AGENT);
    if (j == 10) {
      for (unsigned spins = 0; __builtin_amdgcn_readfirstlane(seen) != 3u && spins < (1u << 22); ++spins) {
        __builtin_amdgcn_s_sleep(1);
        seen = __hip_atomic_load(cnt + (T * 4 + p) * 32, __ATOMIC_RELAXED, __HIP_MEMORY_SCOPE_AGENT);
      }
      __builtin_amdgcn_fence(__ATOMIC_ACQUIRE, "wavefront");
      asm volatile("" ::: "memory");
#pragma unroll
      for (int s = 0; s < 3; ++s) {
        const unsigned po = (unsigned)(((T * 4 + p) * 4 + (p ^ (s + 1))) * 16384 + lane * 16);
#pragma unroll
        for (int a = 0; a < 2; ++a)
#pragma unroll
          for (int b = 0; b < 2; ++b)
            pp[s][a][b] = __builtin_amdgcn_raw_buffer_load_b128(prs, po + (((w & 1) * 2 + a) * 4 + (w >> 1) * 2 + b) * 1024, 0, 16);
      }
#pragma unroll
      for (int b = 0; b < 2; ++b)
        xv[b] = *(const u32x4*)(x + (size_t)(t0 + (w >> 1) * 32 + b * 16 + fi) * D + e0 + (w & 1) * 32 + g * 8);
      b2v = *(const u32x4*)(b2 + e0 + (w & 1) * 32 + g * 8);
    }
    if (j < K2_NB - 1) loadf(0, (j + 1) & 3, 0);
    domma(1);
    SCHED0();
  }
  const float sgn = p < 2 ? 1.f : -1.f;
#pragma unroll
  for (int b = 0; b < 2; ++b) {
    float res[8];
#pragma unroll
    for (int a = 0; a < 2; ++a) {
      f32x4 own = *(const LAS f32x4*)(uintptr_t)(lbase + K2_OWN + (((w & 1) * 2 + a) * 4 + (w >> 1) * 2 + b) * 1024 + lane * 16);
      f32x4 ys = own + __builtin_bit_cast(f32x4, pp[0][a][b]);
      f32x4 yo = __builtin_bit_cast(f32x4, pp[1][a][b]) + __builtin_bit_cast(f32x4, pp[2][a][b]);
#pragma unroll
      for (int j = 0; j < 4; ++j) {
        float y = sgn * rbf(rbf(ys[j]) - rbf(yo[j]));
        unsigned bw = b2v[a * 2 + (j >> 1)], xw = xv[b][a * 2 + (j >> 1)];
        float bb = (j & 1) ? bfhi(bw) : bflo(bw);
        float xx = (j & 1) ? bfhi(xw) : bflo(xw);
        float o = rbf(rbf(acc[a][b][j]) + bb);
        float gg = rbf(o / (1.f + __expf(-o)));
        res[a * 4 + j] = xx + rbf(y * gg);
      }
    }
    u32x4 o = {pack2(res[0], res[1]), pack2(res[2], res[3]), pack2(res[4], res[5]), pack2(res[6], res[7])};
    *(u32x4*)(out + (size_t)(t0 + (w >> 1) * 32 + b * 16 + fi) * D + e0 + (w & 1) * 32 + g * 8) = o;
  }
}

extern "C" void kernel_launch(void* const* d_in, const int* in_sizes, int n_in,
                              void* d_out, int out_size, void* d_ws, size_t ws_size,
                              hipStream_t stream) {
  const u16* x = (const u16*)d_in[0];
  const u16* cr = (const u16*)d_in[1];
  const u16* ci = (const u16*)d_in[2];
  const u16* dr = (const u16*)d_in[3];
  const u16* di = (const u16*)d_in[4];
  const u16* w1 = (const u16*)d_in[6];
  const u16* b1 = (const u16*)d_in[7];
  const u16* w2 = (const u16*)d_in[8];
  const u16* b2 = (const u16*)d_in[9];
  k1_kernel<<<256, 512, 0, stream>>>(x, dr, di, w1, b1, w2, (char*)d_ws);
  k2_kernel<<<256, 512, 0, stream>>>(x, cr, ci, b2, (char*)d_ws, (u16*)d_out);
}
```

```cpp
#include <hip/hip_runtime.h>
#include <hip/hip_bf16.h>
#include <stdint.h>

typedef unsigned short u16;
typedef __attribute__((ext_vector_type(8))) __bf16 bf16x8;
typedef __attribute__((ext_vector_type(2))) __bf16 bf16x2;
typedef __attribute__((ext_vector_type(4))) short s16x4;
typedef __attribute__((ext_vector_type(4))) float f32x4;
typedef __attribute__((ext_vector_type(4))) unsigned u32x4;
typedef __attribute__((ext_vector_type(2))) unsigned u32x2;
#define LAS __attribute__((address_space(3)))
#define GAS __attribute__((address_space(1)))
#define DEVI __device__ __forceinline__
#define U2F(x) __builtin_bit_cast(float, (unsigned)(x))
#define F2U(x) __builtin_bit_cast(unsigned, (float)(x))
#define ALL(x) (__builtin_amdgcn_ballot_w64(!(x)) == 0ull)
#define RLX_AGENT __ATOMIC_RELAXED, __HIP_MEMORY_SCOPE_AGENT

constexpr int S = 2048, D = 512, F = 1792;
constexpr int NFB = F / 32;
constexpr int NSB = S / 32;

constexpr unsigned WS_ABF = 1u << 20;
constexpr unsigned WS_HF = 5u << 20;
constexpr unsigned WS_PART = 12u << 20;
constexpr unsigned WS_FA = 20u << 20;
constexpr unsigned WS_FH = WS_FA + 4096;
constexpr unsigned WS_FP = WS_FA + 8192;
constexpr unsigned WS_EP = WS_FA + 8192 + 32768;
constexpr unsigned WS_ST = WS_EP + 4096;
constexpr unsigned WS_END = WS_ST + 32768;

#if defined(__HIP_DEVICE_COMPILE__)
#pragma clang attribute push(__attribute__((target("no-packed-fp32-ops"))), apply_to = function)
#endif
DEVI float bflo(unsigned v) { return U2F(v << 16); }
DEVI float bfhi(unsigned v) { return U2F(v & 0xffff0000u); }
DEVI unsigned pack2(float a, float b) {
  bf16x2 r; r[0] = (__bf16)a; r[1] = (__bf16)b; return __builtin_bit_cast(unsigned, r);
}
DEVI float rbf(float f) { return (float)(__bf16)f; }
DEVI f32x4 mfma16(u32x4 a, u32x4 b, f32x4 c) {
  return __builtin_amdgcn_mfma_f32_16x16x32_bf16(__builtin_bit_cast(bf16x8, a), __builtin_bit_cast(bf16x8, b), c, 0, 0, 0);
}
#define SC1 " sc1"
#define GL_(v, s, off, pol) "global_load_lds_dwordx4 " v ", " s " offset:" #off pol "\n\t"
#define GL4(pol, v0, v1, v2, v3, sb, lds)                                                                   \
  asm volatile("s_mov_b32 m0, %5\n\ts_nop 0\n\t" GL_("%0", "%4", 0, pol) GL_("%1", "%4", 1024, pol)         \
               GL_("%2", "%4", 2048, pol) GL_("%3", "%4", 3072, pol)                                        \
               :: "v"(v0), "v"(v1), "v"(v2), "v"(v3), "s"(sb), "s"(lds) : "memory")
#define GL3(pol, v0, v1, v2, sb, lds)                                                                       \
  asm volatile("s_mov_b32 m0, %4\n\ts_nop 0\n\t" GL_("%0", "%3", 0, pol) GL_("%1", "%3", 1024, pol)         \
               GL_("%2", "%3", 2048, pol) :: "v"(v0), "v"(v1), "v"(v2), "s"(sb), "s"(lds) : "memory")
#define GL2(pol, v0, v1, sb, lds)                                                                           \
  asm volatile("s_mov_b32 m0, %3\n\ts_nop 0\n\t" GL_("%0", "%2", 0, pol) GL_("%1", "%2", 1024, pol)         \
               :: "v"(v0), "v"(v1), "s"(sb), "s"(lds) : "memory")
#define GLD1(pol, v0, sb, lds)                                                                              \
  asm volatile("s_mov_b32 m0, %2\n\ts_nop 0\n\tglobal_load_lds_dword %0, %1" pol                            \
               :: "v"(v0), "s"(sb), "s"(__builtin_amdgcn_readfirstlane(lds)) : "memory")
DEVI unsigned poll_load(unsigned voff, const void* sbase) {
  unsigned r;
  asm volatile("global_load_dword %0, %1, %2 sc1\n\ts_waitcnt vmcnt(0)" : "=&v"(r) : "v"(voff), "s"(sbase) : "memory");
  return r;
}
#define WAITV(n) asm volatile("s_waitcnt vmcnt(" #n ")" ::: "memory")
#define BAR_LGKM() asm volatile("s_waitcnt lgkmcnt(0)\n\ts_barrier" ::: "memory")
#define WBAR(n) asm volatile("s_waitcnt vmcnt(" #n ") lgkmcnt(0)\n\ts_barrier" ::: "memory")
#define SCHED0() __builtin_amdgcn_sched_barrier(0)
#define SGB(m, n) __builtin_amdgcn_sched_group_barrier(m, n, 0)
#define PINS(x) asm volatile("" : "+s"(x))
#define PINV(x) asm volatile("" : "+v"(x))
DEVI u32x4 ldsr128(unsigned a) { return *(const LAS u32x4*)(uintptr_t)a; }
DEVI u32x2 ldsr64(unsigned a) { return *(const LAS u32x2*)(uintptr_t)a; }
DEVI unsigned ldsr32(unsigned a) { return *(const LAS unsigned*)(uintptr_t)a; }
DEVI u32x2 ldstr(unsigned a) {
  return __builtin_bit_cast(u32x2, __builtin_amdgcn_ds_read_tr16_b64_v4i16((LAS s16x4*)(uintptr_t)a));
}
template <int CTRL> DEVI float dpp_add(float v) {
  return v + __builtin_bit_cast(float, __builtin_amdgcn_update_dpp(0, __builtin_bit_cast(int, v), CTRL, 0xf, 0xf, true));
}
DEVI float red32(float v) {
  v = dpp_add<0xB1>(v);
  v = dpp_add<0x4E>(v);
  v = dpp_add<0x141>(v);
  v = dpp_add<0x140>(v);
  auto sw = __builtin_amdgcn_permlane16_swap(F2U(v), F2U(v), false, false);
  return U2F(sw[0]) + U2F(sw[1]);
}

constexpr int XB = 16384, ST = 32768;
constexpr int NS = 4;
constexpr int T_OWN = NS * ST;
constexpr int T_ROW = T_OWN + 16384;
constexpr int T_CD = T_ROW + 2048;
constexpr int T_CW = T_CD + 256;
constexpr int T_FLG = T_CW + 1024;
constexpr int T_EP = T_FLG + 3072;
constexpr int T_B1 = T_EP + 256;
constexpr int T_REL = T_B1 + 256;
constexpr int T_VW = T_OWN;
constexpr int T_DUM = T_REL + 64;
constexpr int LDS_BYTES = T_DUM + 256;
constexpr int NA = 16, NB = 14;
constexpr unsigned SPIN_CAP = 1u << 20;
constexpr unsigned BIAS = 4096;
constexpr unsigned SALT = 0x9E3779B9u;
constexpr int FA_KS = 3;
constexpr int FH_J = 7;
constexpr int FHS = 11;
constexpr int FP_J = 6;
constexpr int ST_J = 4;
#define KA(j) ((j) < 8 ? (((j) + 2 * te) & 7) : (((j) & 12) | (((j) + te) & 3)))

__global__ __launch_bounds__(512, 2) void fused_kernel(const u16* __restrict__ x, const u16* __restrict__ cr,
                                                       const u16* __restrict__ ci, const u16* __restrict__ dr,
                                                       const u16* __restrict__ di, const u16* __restrict__ w1,
                                                       const u16* __restrict__ b1, const u16* __restrict__ w2,
                                                       const u16* __restrict__ b2, char* ws, u16* __restrict__ out) {
  __shared__ __attribute__((aligned(16))) char smem[LDS_BYTES];
  const unsigned lbase = (unsigned)(uintptr_t)(LAS char*)smem;
  const int bid = __builtin_amdgcn_workgroup_id_x();
  const int xcd = bid & 7, li = bid >> 3;
  const int r = (xcd & 1) * 8 + (li & 7);
  const int p = xcd >> 1, te = li >> 3;
  const int c = p * 4 + te;
  const int T = r * 4 + te;
  const int t0 = r * 128 + (p >> 1) * 64, e0 = te * 128 + (p & 1) * 64;
  const int wave = __builtin_amdgcn_readfirstlane(__builtin_amdgcn_workitem_id_x() >> 6) ^ 4;
  const int lane = __builtin_amdgcn_workitem_id_x() & 63;
  const int fi = lane & 15, g = lane >> 4;
  const int codd = c & 1;
  const int rotb = (r & 7) * 3 / 2, rotp = r & 3;
  unsigned V;

  if (__builtin_expect(wave < 4, 1)) {
    const int lw = wave;
    asm volatile("" :: "s"(x), "s"(cr), "s"(ci), "s"(dr), "s"(di), "s"(w1), "s"(b1), "s"(w2), "s"(ws));
    const unsigned l3 = (unsigned)lane >> 3, l7 = (unsigned)lane & 7;
    const unsigned rowp = (l3 >> 2) * 8 + (l3 & 3);
    const unsigned che = (l7 ^ (l3 >> 1)) * 16;
    const char* xm = (const char*)x + (r * 128 + lw * 32) * 1024 - BIAS;
    const char* dm = (const char*)(c < 8 ? dr : di) + ((c & 7) * 64 + lw * 16) * 1024 - BIAS;
    unsigned vx[4], vd[2];
#pragma unroll
    for (int i = 0; i < 4; ++i) vx[i] = rowp * 1024 + (che ^ ((i & 1) * 64)) + (((i & 1) * 16 + (i >> 1) * 4) * 1024 + BIAS - i * 1024);
#pragma unroll
    for (int i = 0; i < 2; ++i) vd[i] = l3 * 1024 + (che ^ (i * 64)) + (i * 8 * 1024 + BIAS - i * 1024);
    auto issueX = [&](int j) {
      const unsigned sl = lbase + (unsigned)(j & 3) * ST;
      GL4("", vx[0], vx[1], vx[2], vx[3], xm + ((j + 2 * rotp) & 7) * 128, sl + lw * 4096);
      GL2("", vd[0], vd[1], dm + ((j + 2 * rotp) & 7) * 128, sl + XB + lw * 2048);
    };
    issueX(0);
    __builtin_amdgcn_s_setprio(3);
    issueX(1); issueX(2); issueX(3);
    if (lw == 0) GLD1(SC1, 0u, ws + WS_EP, lbase + T_EP);
    if (lw == 1) {
      int tl = (lane >> 3) + codd; if (tl >= 7) tl -= 7;
      GLD1("", (unsigned)((c * 112 + tl * 16 + (lane & 7) * 2) * 2), b1, lbase + T_B1);
    }
    unsigned vc[4], va[4], v2[4];
    u32x4 vwa, vwb;
    const unsigned vh = (unsigned)lane * 16;
    const char* wm = (const char*)w1 + (size_t)lw * 32 * F * 2 + c * 224 - BIAS;
    const char* cm = (const char*)(p < 2 ? cr : ci) + (size_t)(r * 128 + lw * 32) * 4096 + (p & 1) * 2048 - BIAS;
    const char* am = ws + WS_ABF + (((p >> 1) * 32 + te * 8 + lw * 2) * NSB + (p & 1) * 32) * 1024 - BIAS;
    auto issueW = [&](int j) {
      const unsigned sl = lbase + (unsigned)(j & 3) * ST + lw * 7168;
      const char* b = wm + (size_t)((j + rotp) & 3) * 128 * F * 2;
      GL4("", vwa[0], vwa[1], vwa[2], vwa[3], b, sl);
      GL3("", vwb[0], vwb[1], vwb[2], b, sl + 4096);
    };
    auto issueC = [&](int j) {
      GL4("", vc[0], vc[1], vc[2], vc[3], cm + KA(j) * 128, lbase + (unsigned)(j & 3) * ST + lw * 4096);
    };
    auto issueA = [&](int j) {
      GL4(SC1, va[0], va[1], va[2], va[3], am + KA(j) * 2048, lbase + (unsigned)(j & 3) * ST + lw * 4096 + 16384);
    };
    u32x4 ones = {0x3f803f80u, 0x3f803f80u, 0x3f803f80u, 0x3f803f80u};
    PINV(ones);
    unsigned ad = lbase + XB + (unsigned)((lw * 16 + fi) * 128 + ((g ^ (fi >> 1)) * 16));
    PINV(ad);
    {
      f32x4 cd0 = {0, 0, 0, 0}, cd1 = {0, 0, 0, 0};
#pragma unroll
      for (int j = 0; j < 8; ++j) {
        if (j == 0) WBAR(18); else WBAR(12);
        unsigned a = ad + (unsigned)(j & 3) * ST;
        PINV(a);
        u32x4 d0 = ldsr128(a), d1 = ldsr128(a ^ 64u);
        if (j == 0) { vwa = ldsr128(lbase + T_VW + vh); vwb = ldsr128(lbase + T_VW + 1024 + vh); }
        if (j >= 1 && j < 5) issueX(j + 3); else if (j == 5) issueW(0);
        if (j == 6) issueW(1);
        cd0 = mfma16(ones, d0, cd0); cd1 = mfma16(ones, d1, cd1);
      }
      *(LAS float*)(uintptr_t)(lbase + T_CD + (lw * 16 + fi) * 4) = cd0[0] + cd1[0];
    }
    unsigned at, aflg, vl4;
    const char* fam = ws + WS_FA + (p * 4 + te) * 256;
    const bool mine = lane < 32 && ((((lane >> 3) - te) & 3) < 2);
    {
      f32x4 cw[2] = {{0, 0, 0, 0}, {0, 0, 0, 0}};
      const unsigned t1 = lw < 3 ? 32u : 0u;
      auto colsum = [&](int j) {
        unsigned a0 = at + (unsigned)j * ST;
        PINV(a0);
        const unsigned a1 = a0 + t1;
#pragma unroll
        for (int k = 0; k < 4; ++k) {
          u32x2 lo = ldstr(a0 + k * 32 * 224), hi = ldstr(a0 + (k * 32 + 8) * 224);
          u32x2 lo1 = ldstr(a1 + k * 32 * 224), hi1 = ldstr(a1 + (k * 32 + 8) * 224);
          cw[0] = mfma16((u32x4){lo[0], lo[1], hi[0], hi[1]}, ones, cw[0]);
          cw[1] = mfma16((u32x4){lo1[0], lo1[1], hi1[0], hi1[1]}, ones, cw[1]);
        }
      };
      WBAR(7);
      issueW(2); issueW(3);
      SCHED0();
      at = lbase + (unsigned)(((g >> 1) * 16 + (g & 1) * 4 + (fi >> 2)) * 224 + (fi & 3) * 8 + lw * 64);
      aflg = lbase + T_FLG + (unsigned)(lw * 256 + lane * 4);
      vl4 = (unsigned)lane * 4;
#pragma unroll
      for (int i = 0; i < 4; ++i) {
        vc[i] = rowp * 4096 + (che ^ ((i & 1) * 64)) + (((i & 1) * 16 + (i >> 1) * 4) * 4096 + BIAS - i * 1024);
        va[i] = (unsigned)lane * 16 + ((i >> 1) * NSB * 1024 + (i & 1) * 1024 + BIAS - i * 1024);
        v2[i] = l3 * 1024 + (l7 ^ (l3 & 6)) * 16 + (i * 8192 + BIAS - i * 1024);
        PINV(vc[i]); PINV(va[i]); PINV(v2[i]);
      }
      colsum(0);
      V = ldsr32(lbase + T_EP) + SALT;
      *(LAS unsigned*)(uintptr_t)aflg = ~V;
      *(LAS unsigned*)(uintptr_t)(aflg + 2048) = ~V;
      WBAR(0);
      issueC(0);
      colsum(1); colsum(2); colsum(3);
#pragma unroll
      for (int t = 0; t < 2; ++t) {
        *(LAS f32x4*)(uintptr_t)(lbase + T_CW + ((lw * 2 + t) * 16 + g * 4) * 4) = cw[t];
        u32x2 bw = ldsr64(lbase + T_B1 + (lw * 2 + t) * 32 + g * 8);
        *(LAS f32x4*)(uintptr_t)(lbase + T_CW + 512 + ((lw * 2 + t) * 16 + g * 4) * 4) = (f32x4){bflo(bw[0]), bfhi(bw[0]), bflo(bw[1]), bfhi(bw[1])};
      }
      *(volatile LAS unsigned*)(uintptr_t)(lbase + T_REL + lw * 4) = V;
    }
    auto wait_rel = [&](int grp) {
#pragma nounroll
      for (unsigned spins = 0; spins < SPIN_CAP * 16; ++spins) {
        u32x4 q = *(const volatile LAS u32x4*)(uintptr_t)(lbase + T_REL + grp * 16);
        if (ALL(q[0] == V && q[1] == V && q[2] == V && q[3] == V)) break;
        __builtin_amdgcn_s_sleep(1);
      }
      asm volatile("" ::: "memory");
    };
    if (lw == 0) {
#pragma nounroll
      for (unsigned spins = 0; spins < SPIN_CAP; ++spins) {
        if (ALL(poll_load(vl4, fam) == V || !mine)) break;
        __builtin_amdgcn_s_sleep(8);
      }
      *(volatile LAS u32x4*)(uintptr_t)(lbase + T_REL + 48) = (u32x4){V, V, V, V};
    } else {
      wait_rel(3);
    }
    issueA(0);
    wait_rel(0);
    wait_rel(1);
    issueA(1); issueC(1);
    wait_rel(2);
    issueA(2); issueC(2);
    GLD1(SC1, vl4, fam, lbase + T_FLG + lw * 256);
    WBAR(16);
    issueC(3); issueA(3);
    {
      unsigned f = ldsr32(aflg);
#pragma nounroll
      for (unsigned spins = 0; !ALL(f == V) && spins < SPIN_CAP; ++spins) { __builtin_amdgcn_s_sleep(4); f = poll_load(vl4, fam); }
    }
    const char* hm = ws + WS_HF + ((t0 >> 4) + lw) * NFB * 1024;
    const char* w2m = (const char*)w2 + e0 * 2 - BIAS;
    auto kb = [&](int j) { int k = j + rotb; return j >= 12 ? j : (k >= 12 ? k - 12 : k); };
    auto w2base = [&](int j) {
      const int nb = kb(j) * 4 + lw;
      const int pr6 = (nb * 43) >> 8, ix6 = nb - pr6 * 6;
      const int fb = nb < 48 ? pr6 * 7 + ix6 + (ix6 >= 3) : (nb - 48) * 7 + 3;
      return w2m + fb * 32768;
    };
    {
      const char* fhm = ws + WS_FH + (r * 2 + (p >> 1)) * 128;
      auto stepA = [&](int s) {
        const int k = KA(s + 3);
        const char* b0 = cm + k * 128;
        const char* b1p = am + k * 2048;
        unsigned sl1 = lbase + (unsigned)((s + 3) & 3) * ST + lw * 4096, sl2 = sl1 + 16384;
        PINS(b0); PINS(b1p); PINS(sl1); PINS(sl2);
        WBAR(16);
        GL4("", vc[0], vc[1], vc[2], vc[3], b0, sl1);
        GL4(SC1, va[0], va[1], va[2], va[3], b1p, sl2);
      };
#pragma nounroll
      for (int s = 1; s < FHS; ++s) stepA(s);
      GLD1(SC1, vl4 & 127u, fhm, lbase + T_FLG + 2048 + lw * 256);
#pragma nounroll
      for (int s = FHS; s < 13; ++s) stepA(s);
      unsigned f = ldsr32(aflg + 2048);
#pragma nounroll
      for (unsigned spins = 0; !ALL(f == V) && spins < SPIN_CAP; ++spins) { __builtin_amdgcn_s_sleep(4); f = poll_load(vl4 & 127u, fhm); }
    }
#pragma nounroll
    for (int j = 0; j < NB; ++j) {
      unsigned sl1 = lbase + (unsigned)(j & 3) * ST + lw * 4096, sl2 = sl1 + 16384;
      const char* b0 = hm + kb(j) * 4096;
      const char* b1p = w2base(j);
      PINS(b0); PINS(b1p); PINS(sl1); PINS(sl2);
      WBAR(16);
      GL4(SC1, vh, vh, vh, vh, b0, sl1);
      GL4("", v2[0], v2[1], v2[2], v2[3], b1p, sl2);
    }
    WBAR(16);
    WBAR(8);
    WBAR(0);
    if (bid == 0 && lw == 0) {
#pragma nounroll
      for (unsigned spins = 0; spins < SPIN_CAP; ++spins) {
        u32x4 f0, f1, f2, f3;
        asm volatile("global_load_dwordx4 %0, %4, %5 sc1\n\tglobal_load_dwordx4 %1, %4, %5 offset:1024 sc1\n\t"
                     "global_load_dwordx4 %2, %4, %5 offset:2048 sc1\n\tglobal_load_dwordx4 %3, %4, %5 offset:3072 sc1\n\ts_waitcnt vmcnt(0)"
                     : "=&v"(f0), "=&v"(f1), "=&v"(f2), "=&v"(f3) : "v"(vh), "s"(ws + WS_FH) : "memory");
        u32x4 d = (f0 ^ V) | (f1 ^ V) | (f2 ^ V) | (f3 ^ V);
        if (ALL((d[0] | d[1] | d[2] | d[3]) == 0u)) break;
      }
      if (lane == 0) __hip_atomic_store((GAS unsigned*)(ws + WS_EP), V, RLX_AGENT);
    }
    return;
  }

  const int w = wave - 4;
  asm volatile("" :: "s"(x), "s"(cr), "s"(ci), "s"(dr), "s"(di), "s"(w1), "s"(b1), "s"(w2), "s"(ws));
  __builtin_amdgcn_s_setprio(3);
  const unsigned sw = (unsigned)(fi >> 1);
  const __amdgpu_buffer_rsrc_t wrs = __builtin_amdgcn_make_buffer_rsrc(ws, 0, WS_END, 0x00020000);
  {
    const int row = r * 128 + c * 8 + w * 2 + (lane >> 5);
    u32x4 x0, x1;
    asm volatile("global_load_dwordx4 %0, %3, %4\n\tglobal_load_dwordx4 %1, %3, %4 offset:512\n\tglobal_load_dword %2, %5, %6 sc1"
                 : "=&v"(x0), "=&v"(x1), "=&v"(V) : "v"((unsigned)(row * 1024 + (lane & 31) * 16)), "s"(x), "v"(0u), "s"(ws + WS_EP) : "memory");
    __builtin_amdgcn_s_setprio(0);
    unsigned sdum;
    {
      const char* pa = (const char*)w2 + (bid * 4 + w) * 1792;
      asm volatile("s_load_dword %0, %1, 0x0\n\ts_load_dword %0, %1, 0x80\n\ts_load_dword %0, %1, 0x100\n\ts_load_dword %0, %1, 0x180\n\t"
                   "s_load_dword %0, %1, 0x200\n\ts_load_dword %0, %1, 0x280\n\ts_load_dword %0, %1, 0x300\n\ts_load_dword %0, %1, 0x380\n\t"
                   "s_load_dword %0, %1, 0x400\n\ts_load_dword %0, %1, 0x480\n\ts_load_dword %0, %1, 0x500\n\ts_load_dword %0, %1, 0x580\n\t"
                   "s_load_dword %0, %1, 0x600\n\ts_load_dword %0, %1, 0x680" : "=&s"(sdum) : "s"(pa) : "memory");
    }
    {
      u32x2 vw;
#pragma unroll
      for (int q = 0; q < 2; ++q) {
        const int i = w * 2 + q;
        int o = i * 64 + lane; int rho = (o * 4682) >> 16; int cp = o - rho * 14;
        int k = (rho & ~12) | ((rho & 4) << 1) | ((rho & 8) >> 1);
        int cq = cp + codd * 2; if (cq >= 14) cq -= 14;
        vw[q] = (unsigned)(k * (F * 2) + cq * 16) + (BIAS - (i & 3) * 1024);
      }
      *(LAS u32x2*)(uintptr_t)(lbase + T_VW + (w >> 1) * 1024 + lane * 16 + (w & 1) * 8) = vw;
    }
    asm volatile("s_waitcnt vmcnt(0) lgkmcnt(0)" : "+v"(x0), "+v"(x1), "+v"(V), "+s"(sdum) :: "memory");
    V += SALT;
    *(LAS unsigned*)(uintptr_t)(lbase + T_FLG + 1024 + w * 256 + lane * 4) = ~V;
    if (lane < 4) *(LAS unsigned*)(uintptr_t)(lbase + T_REL + lane * 16 + w * 4) = ~V;
    float v[16];
#pragma unroll
    for (int i = 0; i < 4; ++i) { v[2 * i] = bflo(x0[i]); v[2 * i + 1] = bfhi(x0[i]); v[8 + 2 * i] = bflo(x1[i]); v[9 + 2 * i] = bfhi(x1[i]); }
    float sm = 0.f;
#pragma unroll
    for (int i = 0; i < 16; ++i) sm += v[i];
    sm = red32(sm);
    const float mean = sm * (1.f / D);
    float sq = 0.f;
#pragma unroll
    for (int i = 0; i < 16; ++i) { float d = v[i] - mean; sq = __builtin_fmaf(d, d, sq); }
    sq = red32(sq);
    const float den = rbf(__builtin_amdgcn_sqrtf(rbf(rbf(sq * (1.f / D)) + 1e-5f)));
    if ((lane & 31) == 0) {
      GAS unsigned long long* gp = (GAS unsigned long long*)(ws + WS_ST) + row * 2;
      __hip_atomic_store(gp, ((unsigned long long)F2U(-rbf(mean)) << 32) | V, RLX_AGENT);
      __hip_atomic_store(gp + 1, ((unsigned long long)F2U(__builtin_amdgcn_rcpf(den)) << 32) | V, RLX_AGENT);
    }
  }
  f32x4 ah[7][2];
  {
    u32x4 xf[16][2];
    const unsigned atr = lbase + (unsigned)(((g >> 1) * 16 + (g & 1) * 4 + (fi >> 2)) * 224 + (fi & 3) * 8);
    u32x4 wf[2][7];
    auto loadw = [&](int ks) {
      unsigned at = atr + (unsigned)(ks >> 2) * ST;
      PINV(at);
#pragma unroll
      for (int t = 0; t < 7; ++t) {
        u32x2 lo = ldstr(at + ((ks & 3) * 32) * 224 + t * 32);
        u32x2 hi = ldstr(at + ((ks & 3) * 32 + 8) * 224 + t * 32);
        wf[ks & 1][t] = (u32x4){lo[0], lo[1], hi[0], hi[1]};
      }
    };
    {
      f32x4 aab[4][2];
#pragma unroll
      for (int n = 0; n < 4; ++n) { aab[n][0] = (f32x4){0, 0, 0, 0}; aab[n][1] = (f32x4){0, 0, 0, 0}; }
      const unsigned ar0 = lbase + (unsigned)(fi * 128) + ((g ^ sw) * 16);
      u32x4 df[2][4];
      auto loadf = [&](int ks) {
        unsigned ar = (ar0 ^ (unsigned)((ks & 1) * 64)) + (unsigned)((ks >> 1) & 3) * ST;
        PINV(ar);
        unsigned arx = ar + (unsigned)(w * 4096);
#pragma unroll
        for (int u = 0; u < 2; ++u) xf[ks][u] = ldsr128(arx + u * 2048);
#pragma unroll
        for (int n = 0; n < 4; ++n) df[ks & 1][n] = ldsr128(ar + XB + n * 2048);
      };
      auto domma = [&](int ks) {
#pragma unroll
        for (int n = 0; n < 4; ++n)
#pragma unroll
          for (int u = 0; u < 2; ++u) aab[n][u] = mfma16(xf[ks][u], df[ks & 1][n], aab[n][u]);
      };
      const unsigned vst = (unsigned)((r * 128 + w * 32 + (lane & 31)) * 16);
      auto fetch_stats = [&]() {
        if (lane < 32)
          asm volatile("s_mov_b32 m0, %2\n\ts_nop 0\n\tglobal_load_lds_dwordx4 %0, %1 sc1"
                       :: "v"(vst), "s"(ws + WS_ST), "s"(__builtin_amdgcn_readfirstlane(lbase + T_ROW + w * 512)) : "memory");
      };
      BAR_LGKM();
      loadf(0);
      SCHED0();
      u32x4 st[8];
      float cs[4];
#pragma unroll
      for (int j = 0; j < 8; ++j) {
        loadf(2 * j + 1);
        domma(2 * j);
#pragma unroll
        for (int i = 0; i < 3; ++i) { SGB(0x100, 2); SGB(0x8, 1); }
        SGB(0x8, 5);
        SCHED0();
        if (j == ST_J) fetch_stats();
        if (j == 7) {
#pragma nounroll
          for (unsigned spins = 0; spins < SPIN_CAP; ++spins) {
            WAITV(0);
            u32x4 q = ldsr128(lbase + T_ROW + w * 512 + (lane & 31) * 16);
            if (ALL(q[0] == V && q[2] == V)) break;
            fetch_stats();
          }
        }
        BAR_LGKM();
        if (j < 7) {
          loadf(2 * j + 2);
        } else {
          unsigned a = lbase + T_ROW + (unsigned)(w * 512 + g * 128), a2 = lbase + T_CD + (unsigned)(fi * 4);
          PINV(a); PINV(a2);
#pragma unroll
          for (int n = 0; n < 4; ++n) cs[n] = U2F(ldsr32(a2 + n * 64));
#pragma unroll
          for (int q = 0; q < 8; ++q) st[q] = ldsr128(a + q * 16);
        }
        domma(2 * j + 1);
        if (j < 7) {
#pragma unroll
          for (int i = 0; i < 3; ++i) { SGB(0x100, 2); SGB(0x8, 1); }
          SGB(0x8, 5);
        } else {
#pragma unroll
          for (int i = 0; i < 6; ++i) { SGB(0x100, 2); SGB(0x8, 1); }
          SGB(0x8, 2);
        }
        SCHED0();
      }
      loadw(0);
      const unsigned ob = WS_ABF + (unsigned)((c * 4 * NSB + r * 4 + w) * 1024 + lane * 16);
#pragma unroll
      for (int n = 0; n < 4; ++n) {
        float v[8];
#pragma unroll
        for (int u = 0; u < 2; ++u)
#pragma unroll
          for (int j = 0; j < 4; ++j) v[u * 4 + j] = __builtin_fmaf(U2F(st[u * 4 + j][1]), cs[n], aab[n][u][j]) * U2F(st[u * 4 + j][3]);
        u32x4 o = {pack2(v[0], v[1]), pack2(v[2], v[3]), pack2(v[4], v[5]), pack2(v[6], v[7])};
        __builtin_amdgcn_raw_buffer_store_b128(o, wrs, ob + n * NSB * 1024, 0, 16);
      }
      SCHED0();
    }
#pragma unroll
    for (int n = 0; n < 7; ++n) { ah[n][0] = (f32x4){0, 0, 0, 0}; ah[n][1] = (f32x4){0, 0, 0, 0}; }
#pragma unroll
    for (int ks = 0; ks < 16; ++ks) {
      if (ks == FA_KS) {
        WAITV(0);
        if (lane == 0)
          __hip_atomic_store((GAS unsigned*)(ws + WS_FA) + (((c >> 3) * 2 + (r >> 3)) * 4 + ((c & 7) >> 1)) * 64 + ((r & 7) * 2 + codd) * 4 + w, V, RLX_AGENT);
      }
      if (ks == 3) BAR_LGKM();
      if (ks == 7 || ks == 11)
        *(volatile LAS unsigned*)(uintptr_t)(lbase + T_REL + (ks >> 2) * 16 + w * 4) = V;
      if (ks < 15) loadw(ks + 1);
#pragma unroll
      for (int t = 0; t < 7; ++t)
#pragma unroll
        for (int u = 0; u < 2; ++u) ah[t][u] = mfma16(wf[ks & 1][t], xf[ks][u], ah[t][u]);
      if (ks < 15) {
#pragma unroll
        for (int i = 0; i < 7; ++i) { SGB(0x100, 2); SGB(0x8, 1); }
        SGB(0x8, 7);
      }
      SCHED0();
    }
  }
  {
  int lane_ = lane;
  PINV(lane_);
  const int lane = lane_, fi = lane & 15, g = lane >> 4;
  const unsigned sw = (unsigned)(fi >> 1);
  const unsigned ahh = lbase + (unsigned)((w >> 1) * 8192 + lane * 16);
  unsigned aw[2];
#pragma unroll
  for (int a = 0; a < 2; ++a)
    aw[a] = lbase + 16384 + (unsigned)((g * 4 + (fi >> 2)) * 128 + ((((w & 1) * 2 + a) ^ ((g * 2 + (fi >> 3)) & 3)) * 32) + (fi & 3) * 8);
  u32x4 hf[2][4][2], wf[2][4][2];
  auto loadb = [&](int buf, unsigned so, bool sp) {
    unsigned a0 = aw[0] + so, a1 = aw[1] + so, a2 = ahh + so;
    asm volatile("" : "+v"(a0), "+v"(a1), "+v"(a2));
#pragma unroll
    for (int k = 0; k < 4; ++k) {
      if (sp) {
        u32x4 h0 = ldsr128(a2 + k * 1024), h1 = ldsr128(a2 + 4096 + k * 1024);
        hf[buf][k][0] = (u32x4){h0[0], h0[1], h1[0], h1[1]};
        hf[buf][k][1] = (u32x4){h0[2], h0[3], h1[2], h1[3]};
      }
#pragma unroll
      for (int n = 0; n < 2; ++n) {
        u32x2 lo = ldstr((n ? a1 : a0) + k * 4096);
        u32x2 hi = ldstr((n ? a1 : a0) + k * 4096 + 2048);
        wf[buf][k][n] = (u32x4){lo[0], lo[1], hi[0], hi[1]};
        if (!sp) hf[buf][k][n] = ldsr128(a2 + n * 4096 + k * 1024);
      }
    }
  };
  u32x2 xv[2][2], b2v[2];
  const unsigned oo = (unsigned)((t0 + (w >> 1) * 32 + (fi >> 2) * 8 + (fi & 3)) * D + e0 + (w & 1) * 32 + g * 4);
  auto load_x = [&]() {
#pragma unroll
    for (int a = 0; a < 2; ++a) {
#pragma unroll
      for (int b = 0; b < 2; ++b) xv[a][b] = *(const u32x2*)(x + oo + b * 4 * D + a * 16);
      b2v[a] = *(const u32x2*)(b2 + e0 + (w & 1) * 32 + a * 16 + g * 4);
    }
  };
  f32x4 acc[4][4];
  {
#pragma unroll
    for (int a = 0; a < 4; ++a)
#pragma unroll
      for (int b = 0; b < 4; ++b) acc[a][b] = (f32x4){0, 0, 0, 0};
    const unsigned ac0 = lbase + (unsigned)(((w >> 1) * 64 + fi) * 128) + ((g ^ sw) * 16);
    const unsigned aa = lbase + 16384 + (unsigned)((w & 1) * 8192 + lane * 16);
    u32x4 cf[2][4], af[2][4];
    auto loadf = [&](int buf, unsigned so, int kk) {
      unsigned a0 = aa + so, a1 = (ac0 ^ (unsigned)(kk * 64)) + so;
      asm volatile("" : "+v"(a0), "+v"(a1));
#pragma unroll
      for (int n = 0; n < 4; ++n) { af[buf][n] = ldsr128(a0 + n * 2048 + kk * 1024); cf[buf][n] = ldsr128(a1 + n * 2048); }
    };
    auto domma = [&](int buf) {
#pragma unroll
      for (int a = 0; a < 4; ++a)
#pragma unroll
        for (int b = 0; b < 4; ++b) acc[a][b] = mfma16(af[buf][a], cf[buf][b], acc[a][b]);
    };
    auto interleave = [&](bool valu) {
#pragma unroll
      for (int i = 0; i < 8; ++i) { SGB(0x100, 1); SGB(0x8, 1); if (valu) SGB(0x2, 1); }
      if (valu) {
#pragma unroll
        for (int i = 0; i < 8; ++i) { if (i < 2) SGB(0x100, 1); SGB(0x8, 1); SGB(0x2, 2); }
      } else {
        SGB(0x8, 8);
      }
    };
    const unsigned hb = WS_HF + (unsigned)(((r * 8 + w * 2) * NFB + c * 3) * 1024 + lane * 16);
    const unsigned hbs = WS_HF + (unsigned)(((r * 8 + w * 2 + codd) * NFB + 48 + (c >> 1)) * 1024 + lane * 16);
    u32x4 st[2];
    f32x4 cs[7], bb[7];
    unsigned a2 = lbase + T_CW + (unsigned)(g * 16);
    auto htab = [&](int t) {
      cs[t] = __builtin_bit_cast(f32x4, ldsr128(a2 + t * 64));
      bb[t] = __builtin_bit_cast(f32x4, ldsr128(a2 + 512 + t * 64));
    };
    {
      unsigned a = lbase + T_ROW + (unsigned)(w * 512 + ((fi >> 2) * 8 + (fi & 3)) * 16);
      PINV(a); PINV(a2);
      st[0] = ldsr128(a); st[1] = ldsr128(a + 64);
    }
    u32x2 hv[7][2];
    auto htile = [&](int t) {
      if (t < 6) htab(t + 1);
#pragma unroll
      for (int u = 0; u < 2; ++u) {
        f32x4 hh = __builtin_elementwise_fma(cs[t], (f32x4)(U2F(st[u][1])), ah[t][u]);
        hh = __builtin_elementwise_fma(hh, (f32x4)(U2F(st[u][3])), bb[t]);
        hh = __builtin_elementwise_max(hh, (f32x4)(0.f));
        hh = hh * hh;
        hv[t][u] = (u32x2){pack2(hh[0], hh[1]), pack2(hh[2], hh[3])};
      }
    };
    auto hstore = [&](int i) {
      if (i < 6) {
        const int pr = i >> 1, u = i & 1;
        __builtin_amdgcn_raw_buffer_store_b128((u32x4){hv[2 * pr][u][0], hv[2 * pr][u][1], hv[2 * pr + 1][u][0], hv[2 * pr + 1][u][1]},
                                               wrs, hb + (u * NFB + pr) * 1024, 0, 16);
      } else {
        __builtin_amdgcn_raw_buffer_store_b128((u32x4){hv[6][0][0], hv[6][0][1], hv[6][1][0], hv[6][1][1]}, wrs, hbs, 0, 16);
      }
    };
    unsigned so = 0;
    auto stage = [&](int j, bool last) {
      loadf(1, so, 1);
      domma(0);
      if (j >= 0 && j < 4) { htile(2 * j); PINV(hv[2 * j][0]); PINV(hv[2 * j][1]); }
      interleave(j >= 0 && j < 4);
      SCHED0();
      if (j >= 1 && j < 4) hstore(2 * j - 1);
      if (j == 4) hstore(6);
      BAR_LGKM();
      so = (so + ST) & (NS * ST - 1);
      if (last) loadb(0, so, false); else loadf(0, so, 0);
      domma(1);
      if (j >= 0 && j < 3) { htile(2 * j + 1); PINV(hv[2 * j + 1][0]); PINV(hv[2 * j + 1][1]); }
      if (last) {
#pragma unroll
        for (int i = 0; i < 12; ++i) { SGB(0x100, 2); SGB(0x8, 1); }
        SGB(0x8, 4);
      } else {
        interleave(j >= 0 && j < 3);
      }
      SCHED0();
      if (j >= 0 && j < 3) hstore(2 * j);
      SCHED0();
    };
    BAR_LGKM();
    loadf(0, 0, 0);
    htab(0);
    SCHED0();
#pragma unroll
    for (int j = 0; j < 5; ++j) stage(j, false);
#pragma nounroll
    for (int j = 5; j < FH_J; ++j) stage(-1, false);
    WAITV(0);
    if (lane == 0) __hip_atomic_store((GAS unsigned*)(ws + WS_FH) + (r * 2 + (w >> 1)) * 32 + c * 2 + (w & 1), V, RLX_AGENT);
    load_x();
    SCHED0();
#pragma nounroll
    for (int j = FH_J; j < NA - 1; ++j) stage(-1, false);
    stage(-1, true);
  }
  f32x4 aco[2][2];
#pragma unroll
  for (int a = 0; a < 2; ++a)
#pragma unroll
    for (int b = 0; b < 2; ++b) aco[a][b] = (f32x4){0, 0, 0, 0};
  auto domma = [&](int buf) {
#pragma unroll
    for (int k = 0; k < 4; ++k)
#pragma unroll
      for (int a = 0; a < 2; ++a)
#pragma unroll
        for (int b = 0; b < 2; ++b) aco[a][b] = mfma16(wf[buf][k][a], hf[buf][k][b], aco[a][b]);
  };
  const unsigned po = WS_PART + (unsigned)(((T * 4 + w) * 4 + p) * 8192 + lane * 16);
  auto pout = [&](int a) {
    if (w == p) {
#pragma unroll
      for (int b = 0; b < 4; ++b) *(LAS f32x4*)(uintptr_t)(lbase + T_OWN + (a * 4 + b) * 1024 + lane * 16) = acc[a][b];
    } else {
#pragma unroll
      for (int b = 0; b < 2; ++b) {
        u32x4 o = {pack2(acc[a][2 * b][0], acc[a][2 * b][1]), pack2(acc[a][2 * b][2], acc[a][2 * b][3]),
                   pack2(acc[a][2 * b + 1][0], acc[a][2 * b + 1][1]), pack2(acc[a][2 * b + 1][2], acc[a][2 * b + 1][3])};
        __builtin_amdgcn_raw_buffer_store_b128(o, wrs, po + (a * 2 + b) * 1024, 0, 16);
      }
    }
  };
  unsigned so = 0;
  auto stage = [&](int buf, bool last, int pst, bool spn) {
    if (!last) {
      BAR_LGKM();
      so = (so + ST) & (NS * ST - 1);
      loadb(buf ^ 1, so, spn);
    }
    domma(buf);
    if (!last) {
#pragma unroll
      for (int i = 0; i < 12; ++i) { SGB(0x100, 2); SGB(0x8, 1); }
      SGB(0x8, 4);
    }
    SCHED0();
    if (pst >= 0) { pout(pst * 2); pout(pst * 2 + 1); }
  };
  stage(0, false, 0, false);
  stage(1, false, 1, false);
  const unsigned vfp = (unsigned)((T * 4 + p) * 128 + (lane & 3) * 4);
#pragma nounroll
  for (int j = 2; j < 10; j += 2) {
    if (j == FP_J) {
      WAITV(0);
      if (lane == 0 && w != p) __hip_atomic_store((GAS unsigned*)(ws + WS_FP) + (T * 4 + w) * 32 + p, V, RLX_AGENT);
    }
    if (j == 8) GLD1(SC1, vfp, ws + WS_FP, lbase + T_FLG + 1024 + w * 256);
    stage(0, false, -1, false);
    stage(1, false, -1, false);
  }
  stage(0, false, -1, false);
  unsigned seen = ldsr32(lbase + T_FLG + 1024 + w * 256 + lane * 4);
  const bool early = ALL(seen == V || (lane & 3) == p);
  u32x4 pp[3][2];
  auto load_pp = [&]() {
#pragma unroll
    for (int s = 0; s < 3; ++s) {
      const unsigned pi = WS_PART + (unsigned)(((T * 4 + p) * 4 + (p ^ (s + 1))) * 8192 + lane * 16);
#pragma unroll
      for (int a = 0; a < 2; ++a)
        pp[s][a] = __builtin_amdgcn_raw_buffer_load_b128(wrs, pi + (((w & 1) * 2 + a) * 2 + (w >> 1)) * 1024, 0, 16);
    }
  };
  asm volatile("" ::: "memory");
  if (early) load_pp();
  SCHED0();
  stage(1, false, -1, true);
  stage(0, false, -1, true);
  stage(1, true, -1, false);
  if (!early) {
#pragma nounroll
    for (unsigned spins = 0; spins < SPIN_CAP; ++spins) {
      seen = poll_load(vfp, ws + WS_FP);
      if (ALL(seen == V || (lane & 3) == p)) break;
    }
    load_pp();
  }
  const float sgn = p < 2 ? 1.f : -1.f;
#pragma unroll
  for (int a = 0; a < 2; ++a) {
    f32x4 bb = {bflo(b2v[a][0]), bfhi(b2v[a][0]), bflo(b2v[a][1]), bfhi(b2v[a][1])};
#pragma unroll
    for (int b = 0; b < 2; ++b) {
      f32x4 own = *(const LAS f32x4*)(uintptr_t)(lbase + T_OWN + (((w & 1) * 2 + a) * 4 + (w >> 1) * 2 + b) * 1024 + lane * 16);
      f32x4 q[3];
#pragma unroll
      for (int s = 0; s < 3; ++s)
        q[s] = (f32x4){bflo(pp[s][a][2 * b]), bfhi(pp[s][a][2 * b]), bflo(pp[s][a][2 * b + 1]), bfhi(pp[s][a][2 * b + 1])};
      f32x4 y = ((own + q[0]) - (q[1] + q[2])) * sgn;
      f32x4 xx = {bflo(xv[a][b][0]), bfhi(xv[a][b][0]), bflo(xv[a][b][1]), bfhi(xv[a][b][1])};
      f32x4 o = aco[a][b] + bb;
      float res[4];
#pragma unroll
      for (int j = 0; j < 4; ++j) {
        float gg = o[j] * __builtin_amdgcn_rcpf(1.f + __builtin_amdgcn_exp2f(o[j] * -1.44269504088896f));
        res[j] = __builtin_fmaf(y[j], gg, xx[j]);
      }
      *(u32x2*)(out + oo + b * 4 * D + a * 16) = (u32x2){pack2(res[0], res[1]), pack2(res[2], res[3])};
    }
  }
  }
}

#if defined(__HIP_DEVICE_COMPILE__)
#pragma clang attribute pop
#endif

extern "C" void kernel_launch(void* const* d_in, const int* in_sizes, int n_in,
                              void* d_out, int out_size, void* d_ws, size_t ws_size,
                              hipStream_t stream) {
  fused_kernel<<<256, 512, 0, stream>>>((const u16*)d_in[0], (const u16*)d_in[1], (const u16*)d_in[2], (const u16*)d_in[3],
                                        (const u16*)d_in[4], (const u16*)d_in[6], (const u16*)d_in[7], (const u16*)d_in[8],
                                        (const u16*)d_in[9], (char*)d_ws, (u16*)d_out);
}
```
